```python
import torch

def _softmax(x, axis=-1):
    return torch.softmax(x, dim=axis)
def _gelu(x):
    return torch.nn.functional.gelu(x, approximate="tanh")
def _silu(x):
    return torch.nn.functional.silu(x)
def _sigmoid(x):
    return torch.sigmoid(x)
def _layernorm(x, axis=-1, eps=1e-5):
    m = x.mean(dim=axis, keepdim=True)
    v = x.var(dim=axis, keepdim=True, unbiased=False)
    return (x - m) / torch.sqrt(v + eps)
def _scatter_add(tgt, idx, src, axis=0):
    return tgt.clone().index_add_(axis, idx, src)
def _segment_sum(src, seg, n):
    out = torch.zeros((n,) + src.shape[1:], dtype=src.dtype)
    return out.index_add_(0, seg, src)
def _pad(x, pw):
    flat = tuple(v for pair in reversed(pw) for v in pair)
    return torch.nn.functional.pad(x, flat)

kernel_name = 'standalone__low_rank_proj_linformer__prompt'

def setup_inputs():

    _w = (lambda a: (lambda t: t.to(torch.bfloat16) if t.is_floating_point() else t)(torch.as_tensor(a)))
    B, H, Sq, Skv, Dh, R = 1, 16, 2048, 2048, 256, 16
    return {
        "q": _w((lambda *s: torch.randn(*s, dtype=torch.bfloat16) * 0.8)(B, H, Sq, Dh)),
        "k": _w((lambda *s: torch.randn(*s, dtype=torch.bfloat16) * 0.8)(B, H, Skv, Dh)),
        "v": _w((lambda *s: torch.randn(*s, dtype=torch.bfloat16) * 0.05)(B, H, Skv, Dh)),
        "e_proj": _w((lambda *s: torch.randn(*s, dtype=torch.bfloat16) * 0.8)(R, Skv) * (Skv ** -0.5)),
        "f_proj": _w((lambda *s: torch.randn(*s, dtype=torch.bfloat16) * 0.05)(R, Skv) * (Skv ** -0.5)),
    }

def reference(q, k, v, e_proj, f_proj):
    B, H, Sq, Dh = q.shape
    kp = torch.einsum("rn,bhnd->bhrd", e_proj, k)
    vp = torch.einsum("rn,bhnd->bhrd", f_proj, v)
    scores = torch.einsum("bhqd,bhrd->bhqr", q, kp) * (Dh ** -0.5)
    p = _softmax(scores, axis=-1)
    o = torch.einsum("bhqr,bhrd->bhqd", p, vp)
    return o
```

```cpp
#include <hip/hip_runtime.h>
#include <hip/hip_bf16.h>
#include <stdint.h>

#define KV_NT 0
#define Q_AUX 0
#define O_AUX 0

typedef __attribute__((ext_vector_type(8))) __bf16 bf16x8;
typedef __attribute__((ext_vector_type(2))) __bf16 bf16x2;
typedef __attribute__((ext_vector_type(4))) short s16x4;
typedef __attribute__((ext_vector_type(8))) short s16x8;
typedef __attribute__((ext_vector_type(4))) float f32x4;
typedef __attribute__((ext_vector_type(2))) float f32x2;
typedef __attribute__((ext_vector_type(4))) unsigned u32x4;
typedef __attribute__((ext_vector_type(2))) unsigned u32x2;
#define LAS __attribute__((address_space(3)))

constexpr int NH = 16, SEQ = 2048, DH = 256, RK = 16;
constexpr unsigned SLAB_BYTES = 131072, FRAG_OFF = 131072, LDS_BYTES = 147456;
constexpr unsigned WS_GRAN_OFF = 65536;
constexpr unsigned SPIN_CAP = 1u << 15;

__device__ __forceinline__ unsigned pack_bf16(float a, float b) {
    bf16x2 r; r[0] = (__bf16)a; r[1] = (__bf16)b;
    return __builtin_bit_cast(unsigned, r);
}
__device__ __forceinline__ float round_bf16(float a) {
    return (float)(__bf16)a;
}
__device__ __forceinline__ float xor16_max(float x) {
    auto r = __builtin_amdgcn_permlane16_swap(__float_as_uint(x), __float_as_uint(x), false, false);
    return fmaxf(__uint_as_float(r[0]), __uint_as_float(r[1]));
}
__device__ __forceinline__ float xor32_max(float x) {
    auto r = __builtin_amdgcn_permlane32_swap(__float_as_uint(x), __float_as_uint(x), false, false);
    return fmaxf(__uint_as_float(r[0]), __uint_as_float(r[1]));
}
__device__ __forceinline__ float xor16_sum(float x) {
    auto r = __builtin_amdgcn_permlane16_swap(__float_as_uint(x), __float_as_uint(x), false, false);
    return __uint_as_float(r[0]) + __uint_as_float(r[1]);
}
__device__ __forceinline__ float xor32_sum(float x) {
    auto r = __builtin_amdgcn_permlane32_swap(__float_as_uint(x), __float_as_uint(x), false, false);
    return __uint_as_float(r[0]) + __uint_as_float(r[1]);
}

template <int T>
__device__ __forceinline__ void proj_steps(const LAS char* tb0, const LAS char* tb1, const u32x4 (&ef)[8], f32x4& a0, f32x4& a1) {
#pragma unroll
    for (int ks = 0; ks < 8; ++ks) {
        switch (ks) {
        case 0: asm volatile("s_waitcnt vmcnt(22)" ::: "memory"); break;
        case 1: asm volatile("s_waitcnt vmcnt(20)" ::: "memory"); break;
        case 2: asm volatile("s_waitcnt vmcnt(18)" ::: "memory"); break;
        case 3: asm volatile("s_waitcnt vmcnt(16)" ::: "memory"); break;
        case 4: asm volatile("s_waitcnt vmcnt(14)" ::: "memory"); break;
        case 5: asm volatile("s_waitcnt vmcnt(12)" ::: "memory"); break;
        case 6: asm volatile("s_waitcnt vmcnt(10)" ::: "memory"); break;
        default: asm volatile("s_waitcnt vmcnt(8)" ::: "memory"); break;
        }
        s16x4 x00 = __builtin_amdgcn_ds_read_tr16_b64_v4i16((LAS s16x4*)(tb0 + ks * 2048));
        s16x4 x01 = __builtin_amdgcn_ds_read_tr16_b64_v4i16((LAS s16x4*)(tb0 + ks * 2048 + 256));
        s16x4 x10 = __builtin_amdgcn_ds_read_tr16_b64_v4i16((LAS s16x4*)(tb1 + ks * 2048));
        s16x4 x11 = __builtin_amdgcn_ds_read_tr16_b64_v4i16((LAS s16x4*)(tb1 + ks * 2048 + 256));
        s16x8 y0 = __builtin_shufflevector(x00, x01, 0, 1, 2, 3, 4, 5, 6, 7);
        s16x8 y1 = __builtin_shufflevector(x10, x11, 0, 1, 2, 3, 4, 5, 6, 7);
        bf16x8 b0 = __builtin_bit_cast(bf16x8, y0), b1 = __builtin_bit_cast(bf16x8, y1);
        bf16x8 e = __builtin_bit_cast(bf16x8, ef[ks]);
        if (T == 0) {
            a0 = __builtin_amdgcn_mfma_f32_16x16x32_bf16(b0, e, a0, 0, 0, 0);
            a1 = __builtin_amdgcn_mfma_f32_16x16x32_bf16(b1, e, a1, 0, 0, 0);
        } else {
            a0 = __builtin_amdgcn_mfma_f32_16x16x32_bf16(e, b0, a0, 0, 0, 0);
            a1 = __builtin_amdgcn_mfma_f32_16x16x32_bf16(e, b1, a1, 0, 0, 0);
        }
    }
}

__global__ __launch_bounds__(512, 2) void linformer_fused(
    const uint16_t* __restrict__ q, const uint16_t* __restrict__ k, const uint16_t* __restrict__ v,
    const uint16_t* __restrict__ ep, const uint16_t* __restrict__ fp, uint16_t* __restrict__ out, unsigned* ws)
{
    __shared__ __attribute__((aligned(16))) char lds[LDS_BYTES];
    const int tid = threadIdx.x, lane = tid & 63;
    const int w = __builtin_amdgcn_readfirstlane(tid >> 6);
    const int b = blockIdx.x;
    const int xcd = b & 7, idx = b >> 3;
    const int h = xcd + 8 * (idx >> 4), j = idx & 15;
    const int t = j >> 3, s = j & 7;
    const int g = lane >> 4, c = lane & 15;

    const __amdgpu_buffer_rsrc_t rW = __builtin_amdgcn_make_buffer_rsrc((void*)ws, 0, WS_GRAN_OFF + 256 * 2048, 0x00020000);
    const __amdgpu_buffer_rsrc_t rX = __builtin_amdgcn_make_buffer_rsrc((void*)((t ? v : k) + (size_t)h * SEQ * DH), 0, SEQ * DH * 2, 0x00020000);
    const __amdgpu_buffer_rsrc_t rE = __builtin_amdgcn_make_buffer_rsrc((void*)(t ? fp : ep), 0, RK * SEQ * 2, 0x00020000);
    const __amdgpu_buffer_rsrc_t rQ = __builtin_amdgcn_make_buffer_rsrc((void*)(q + ((size_t)h * SEQ + 128 * j) * DH), 0, 128 * DH * 2, 0x00020000);
    const __amdgpu_buffer_rsrc_t rO = __builtin_amdgcn_make_buffer_rsrc((void*)(out + ((size_t)h * SEQ + 128 * j) * DH), 0, 128 * DH * 2, 0x00020000);

    const unsigned epoch_raw = __builtin_amdgcn_raw_buffer_load_b32(rW, 0, h * 256, 16);
    u32x4 ef[8];
    {
        const unsigned vo = c * 4096 + g * 16;
#pragma unroll
        for (int ks = 0; ks < 8; ++ks) ef[ks] = __builtin_amdgcn_raw_buffer_load_b128(rE, vo + ks * 64, w * 512, 0);
    }
    const unsigned ldsb = (unsigned)(size_t)lds;
    {
        const unsigned vo = (lane >> 2) * 512 + s * 64 + (((lane & 3) ^ ((lane >> 5) << 1)) * 16);
        const unsigned m0b = ldsb + w * 16384;
        const unsigned sob = w * 131072;
#pragma unroll
        for (int i = 0; i < 16; ++i) {
            asm volatile("s_mov_b32 m0, %0\n\ts_nop 0\n\tbuffer_load_dwordx4 %1, %2, %3 offen lds"
                         :: "s"(m0b + i * 1024), "v"(vo), "s"(rX), "s"(sob + i * 8192) : "m0", "memory");
        }
    }
    u32x4 qf[8];
    {
        const unsigned vo = c * 512 + g * 16;
#pragma unroll
        for (int ks = 0; ks < 8; ++ks) qf[ks] = __builtin_amdgcn_raw_buffer_load_b128(rQ, vo + ks * 64, w * 8192, Q_AUX);
    }
    asm volatile("" ::: "memory");

    f32x4 a0 = {0.f, 0.f, 0.f, 0.f}, a1 = {0.f, 0.f, 0.f, 0.f};
    {
        const unsigned qq = (lane >> 2) & 3, pp = lane & 3;
        const unsigned base = w * 16384 + (8 * g + qq) * 64 + ((((pp >> 1)) ^ (2 * (g & 1))) * 16) + 8 * (pp & 1);
        const LAS char* tb0 = (const LAS char*)lds + base;
        const LAS char* tb1 = (const LAS char*)lds + (base ^ 32);
        if (t == 0) proj_steps<0>(tb0, tb1, ef, a0, a1);
        else proj_steps<1>(tb0, tb1, ef, a0, a1);
    }
    *(LAS f32x4*)((LAS char*)lds + w * 16384 + lane * 16) = a0;
    *(LAS f32x4*)((LAS char*)lds + w * 16384 + 1024 + lane * 16) = a1;
    asm volatile("s_waitcnt lgkmcnt(0)\n\ts_barrier" ::: "memory");

    const unsigned tag = __builtin_amdgcn_readfirstlane(epoch_raw) + 1u;
    if (w < 4) {
        const int i = tid & 3;
        unsigned ro;
        if (t == 0) ro = (w >> 1) * 1024 + (16 * (2 * (w & 1) + (i >> 1)) + ((tid >> 2) & 15)) * 16 + 8 * (i & 1);
        else {
            const int cc = (tid >> 2) & 15, vv = i >> 1;
            ro = (cc >> 3) * 1024 + (16 * w + 8 * ((cc >> 2) & 1) + 4 * vv + (cc & 3)) * 16 + 8 * (i & 1);
        }
        f32x2 sum = *(const LAS f32x2*)((LAS char*)lds + ro);
#pragma unroll
        for (int ww = 1; ww < 8; ++ww) sum += *(const LAS f32x2*)((LAS char*)lds + ww * 16384 + ro);
        u32x2 gr; gr[0] = pack_bf16(sum[0], sum[1]); gr[1] = tag;
        __builtin_amdgcn_raw_buffer_store_b64(gr, rW, tid * 8, WS_GRAN_OFF + (h * 16 + j) * 2048, 16);
    }

    {
        const unsigned vo = w * 4096 + lane * 16;
        const unsigned so = WS_GRAN_OFF + h * 32768;
        u32x4 x[4];
        for (unsigned spin = 0;; ++spin) {
#pragma unroll
            for (int kk = 0; kk < 4; ++kk) x[kk] = __builtin_amdgcn_raw_buffer_load_b128(rW, vo + kk * 1024, so, 16);
            bool ok = true;
#pragma unroll
            for (int kk = 0; kk < 4; ++kk) ok = ok && x[kk][1] == tag && x[kk][3] == tag;
            if (__all(ok) || spin >= SPIN_CAP) break;
            __builtin_amdgcn_s_sleep(2);
        }
#pragma unroll
        for (int kk = 0; kk < 4; ++kk) {
            u32x2 d; d[0] = x[kk][0]; d[1] = x[kk][2];
            *(LAS u32x2*)((LAS char*)lds + FRAG_OFF + w * 2048 + kk * 512 + lane * 8) = d;
        }
    }
    asm volatile("s_waitcnt lgkmcnt(0)\n\ts_barrier" ::: "memory");
    if (j == 0 && tid == 0) __builtin_amdgcn_raw_buffer_store_b32(tag, rW, 0, h * 256, 16);

    f32x4 sc = {0.f, 0.f, 0.f, 0.f};
#pragma unroll
    for (int ks = 0; ks < 8; ++ks) {
        const bf16x8 kp = *(const LAS bf16x8*)((LAS char*)lds + FRAG_OFF + ks * 1024 + lane * 16);
        sc = __builtin_amdgcn_mfma_f32_16x16x32_bf16(kp, __builtin_bit_cast(bf16x8, qf[ks]), sc, 0, 0, 0);
    }
    float sv[4];
#pragma unroll
    for (int r = 0; r < 4; ++r) sv[r] = round_bf16(sc[r]) * 0.0625f;
    float mx = fmaxf(fmaxf(sv[0], sv[1]), fmaxf(sv[2], sv[3]));
    mx = xor32_max(xor16_max(mx));
    float ev[4], sum = 0.f;
#pragma unroll
    for (int r = 0; r < 4; ++r) { ev[r] = __builtin_amdgcn_exp2f((sv[r] - mx) * 1.44269504088896340736f); sum += ev[r]; }
    sum = xor32_sum(xor16_sum(sum));
    const float inv = __builtin_amdgcn_rcpf(sum);
    u32x2 pp2; pp2[0] = pack_bf16(ev[0] * inv, ev[1] * inv); pp2[1] = pack_bf16(ev[2] * inv, ev[3] * inv);
    const s16x4 pb = __builtin_bit_cast(s16x4, pp2);
    const unsigned ovo = c * 512 + g * 16;
#pragma unroll
    for (int u = 0; u < 8; ++u) {
        const u32x4 vf = *(const LAS u32x4*)((LAS char*)lds + FRAG_OFF + (8 + u) * 1024 + lane * 16);
        u32x2 va; va[0] = vf[0]; va[1] = vf[1];
        u32x2 vb; vb[0] = vf[2]; vb[1] = vf[3];
        const f32x4 z = {0.f, 0.f, 0.f, 0.f};
        const f32x4 o0 = __builtin_amdgcn_mfma_f32_16x16x16bf16_1k(__builtin_bit_cast(s16x4, va), pb, z, 0, 0, 0);
        const f32x4 o1 = __builtin_amdgcn_mfma_f32_16x16x16bf16_1k(__builtin_bit_cast(s16x4, vb), pb, z, 0, 0, 0);
        u32x4 ov; ov[0] = pack_bf16(o0[0], o0[1]); ov[1] = pack_bf16(o0[2], o0[3]); ov[2] = pack_bf16(o1[0], o1[1]); ov[3] = pack_bf16(o1[2], o1[3]);
        __builtin_amdgcn_raw_buffer_store_b128(ov, rO, ovo + u * 64, w * 8192, O_AUX);
    }
}

extern "C" void kernel_launch(void* const* d_in, const int* in_sizes, int n_in,
                              void* d_out, int out_size, void* d_ws, size_t ws_size,
                              hipStream_t stream) {
    linformer_fused<<<256, 512, 0, stream>>>((const uint16_t*)d_in[0], (const uint16_t*)d_in[1], (const uint16_t*)d_in[2],
                                             (const uint16_t*)d_in[3], (const uint16_t*)d_in[4], (uint16_t*)d_out, (unsigned*)d_ws);
}
```

```cpp
#include <hip/hip_runtime.h>
#include <hip/hip_bf16.h>
#include <stdint.h>

typedef __attribute__((ext_vector_type(8))) __bf16 bf16x8;
typedef __attribute__((ext_vector_type(2))) __bf16 bf16x2;
typedef __attribute__((ext_vector_type(4))) short s16x4;
typedef __attribute__((ext_vector_type(4))) float f32x4;
typedef __attribute__((ext_vector_type(2))) float f32x2;
typedef __attribute__((ext_vector_type(4))) unsigned u32x4;
typedef __attribute__((ext_vector_type(2))) unsigned u32x2;
#define LAS __attribute__((address_space(3)))

constexpr int SEQ = 2048, DH = 256, RK = 16;
constexpr unsigned EOFF = 131072, LDS_BYTES = 163840, POFF = 8192, KIMG = EOFF, VIMG = EOFF + 8192;
constexpr unsigned WS_GRAN_OFF = 65536, WS_BYTES = WS_GRAN_OFF + 256 * 4096;
constexpr unsigned SPIN_CAP = 1u << 15;
constexpr int WIN = 3;
constexpr int AUX_NT = 2, AUX_SC1 = 16;

__device__ __forceinline__ unsigned pack_bf16(float a, float b) {
    bf16x2 r; r[0] = (__bf16)a; r[1] = (__bf16)b;
    return __builtin_bit_cast(unsigned, r);
}
__device__ __forceinline__ unsigned add_bf16x2(unsigned a, unsigned b) {
    const float lo = __uint_as_float(a << 16) + __uint_as_float(b << 16);
    const float hi = __uint_as_float(a & 0xffff0000u) + __uint_as_float(b & 0xffff0000u);
    return pack_bf16(lo, hi);
}
__device__ __forceinline__ float max_f32(float a, float b) { float r; asm("v_max_f32 %0, %1, %2" : "=v"(r) : "v"(a), "v"(b)); return r; }
__device__ __forceinline__ float groups_max(float x) {
    auto a = __builtin_amdgcn_permlane16_swap(__float_as_uint(x), __float_as_uint(x), false, false);
    x = max_f32(__uint_as_float(a[0]), __uint_as_float(a[1]));
    auto b = __builtin_amdgcn_permlane32_swap(__float_as_uint(x), __float_as_uint(x), false, false);
    return max_f32(__uint_as_float(b[0]), __uint_as_float(b[1]));
}
__device__ __forceinline__ float groups_sum(float x) {
    auto a = __builtin_amdgcn_permlane16_swap(__float_as_uint(x), __float_as_uint(x), false, false);
    x = __uint_as_float(a[0]) + __uint_as_float(a[1]);
    auto b = __builtin_amdgcn_permlane32_swap(__float_as_uint(x), __float_as_uint(x), false, false);
    return __uint_as_float(b[0]) + __uint_as_float(b[1]);
}
#define DMA(pol, m0v, vo, rs, so) \
    asm volatile("s_mov_b32 m0, %0\n\ts_nop 0\n\tbuffer_load_dwordx4 %1, %2, %3 offen " pol " lds" :: "s"(m0v), "v"(vo), "s"(rs), "s"(so) : "memory")
template <int N> __device__ __forceinline__ void waitvm() { asm volatile("s_waitcnt vmcnt(%0)" :: "i"(N) : "memory"); }
#define BARRIER() asm volatile("s_waitcnt lgkmcnt(0)\n\ts_barrier" ::: "memory")

constexpr int PQ = 20;
constexpr int PTOUCH = 12;
constexpr int pos_kind(int p) { return p >= PQ ? 2 : (p == 3 || p == 4 || p == 8 || p == 9) ? 1 : 0; }
constexpr int pos_index(int p) { return p >= PQ ? p - PQ : p == 3 ? 0 : p == 4 ? 1 : p == 8 ? 2 : p == 9 ? 3 : p < 3 ? p : p < 8 ? p - 2 : p - 4; }
constexpr int kv_pos(int i) { return i < 3 ? i : i < 6 ? i + 2 : i + 4; }
constexpr int ks_pos(int ks) { return kv_pos(4 * ks + 3); }

struct Stream {
    unsigned eldsb, m0b, evo, xvo0, xvo1, qvo, eso, xso, qso;
    __amdgpu_buffer_rsrc_t rE, rX, rQ, rO;
};
template <int P> __device__ __forceinline__ void issue(const Stream& s) {
    constexpr int i = pos_index(P);
    if constexpr (pos_kind(P) == 1) DMA("", s.eldsb + i * 1024, s.evo ^ ((i & 1) * 64), s.rE, s.eso + (i & 1) * 32768 + (i >> 1) * 1024);
    else if constexpr (pos_kind(P) == 0) DMA("nt", s.m0b + i * 1024, (i & 1) ? s.xvo1 : s.xvo0, s.rX, s.xso + (512 * (i >> 3) + 8 * (i & 7)) * 512);
    else DMA("nt", s.m0b + i * 1024, s.qvo ^ (i * 32), s.rQ, s.qso + i * 1024);
}

template <int T>
__device__ __forceinline__ void proj_step(const LAS char* xb, unsigned eb, int ks, const unsigned (&to)[4], f32x4 (&acc)[4]) {
    const bf16x8 e = *(const LAS bf16x8*)(size_t)(((ks & 1) ? eb ^ 64 : eb) + (ks >> 1) * 2048);
    bf16x8 x[4];
#pragma unroll
    for (int db = 0; db < 4; ++db) {
        s16x4 x0 = __builtin_amdgcn_ds_read_tr16_b64_v4i16((LAS s16x4*)(xb + to[db] + ks * 4096));
        s16x4 x1 = __builtin_amdgcn_ds_read_tr16_b64_v4i16((LAS s16x4*)(xb + to[db] + ks * 4096 + 512));
        x[db] = __builtin_bit_cast(bf16x8, __builtin_shufflevector(x0, x1, 0, 1, 2, 3, 4, 5, 6, 7));
    }
    __builtin_amdgcn_sched_barrier(0);
#pragma unroll
    for (int db = 0; db < 4; ++db) {
        if (T == 0) acc[db] = __builtin_amdgcn_mfma_f32_16x16x32_bf16(x[db], e, acc[db], 0, 0, 0);
        else        acc[db] = __builtin_amdgcn_mfma_f32_16x16x32_bf16(e, x[db], acc[db], 0, 0, 0);
    }
    __builtin_amdgcn_sched_barrier(0);
}

template <int T, int P>
__device__ __forceinline__ void stream_steps(const Stream& s, const LAS char* xb, unsigned eb, const unsigned (&to)[4], f32x4 (&acc)[4], unsigned (&vt)[2]) {
    if constexpr (P < PQ + WIN) {
        constexpr int need = P - WIN;
        constexpr bool step = need == ks_pos(0) || need == ks_pos(1) || need == ks_pos(2) || need == ks_pos(3);
        if constexpr (step || pos_kind(P) != 1) waitvm<WIN - 1 + (need < 3 ? 1 : 0) + ((P > PTOUCH && need <= PTOUCH) ? 2 : 0)>();
        if constexpr (P == PQ) asm volatile("s_waitcnt lgkmcnt(0)" ::: "memory");
        issue<P>(s);
        if constexpr (P == PTOUCH)
            asm volatile("buffer_load_dword %0, off, %2, 0\n\tbuffer_load_dword %1, off, %3, 0" : "=&v"(vt[0]), "=&v"(vt[1]) : "s"(s.rQ), "s"(s.rO) : "memory");
        if constexpr (need == ks_pos(0)) proj_step<T>(xb, eb, 0, to, acc);
        if constexpr (need == ks_pos(1)) proj_step<T>(xb, eb, 1, to, acc);
        if constexpr (need == ks_pos(2)) proj_step<T>(xb, eb, 2, to, acc);
        if constexpr (need == ks_pos(3)) proj_step<T>(xb, eb, 3, to, acc);
        stream_steps<T, P + 1>(s, xb, eb, to, acc, vt);
    }
}

template <int T>
__device__ __forceinline__ void phase1(LAS char* L, unsigned ldsb, int w, int lane, const Stream& s) {
    static_assert(WIN == 3 && ks_pos(2) + WIN < PQ && ks_pos(1) + WIN < PQ, "the prologue requests positions 0..2");
    const int g = lane >> 4, c = lane & 15;
    const unsigned qq = (lane >> 2) & 3, pp = lane & 3;
    unsigned to[4];
#pragma unroll
    for (int db = 0; db < 4; ++db)
        to[db] = g * 1024 + qq * 128 + (((2 * db + (pp >> 1)) ^ (((g & 1) << 2) | (qq & 2))) * 16) + 8 * (pp & 1);
    const LAS char* xb = L + w * 16384;
    const unsigned eb = ldsb + EOFF + w * 4096 + (c >> 3) * 1024 + (c & 7) * 128 + ((g ^ ((c >> 1) & 7)) * 16);
    f32x4 acc[4];
#pragma unroll
    for (int db = 0; db < 4; ++db) acc[db] = (f32x4){0.f, 0.f, 0.f, 0.f};
    unsigned vt[2];
    stream_steps<T, 3>(s, xb, eb, to, acc, vt);
    asm volatile("" :: "v"(vt[0]), "v"(vt[1]));
#pragma unroll
    for (int db = 0; db < 4; ++db) {
        const unsigned o = T == 0 ? db * 1024 + (g >> 1) * 512 + c * 32 + (g & 1) * 16 : (c & 7) * 528 + (g * 8 + 2 * db + (c >> 3)) * 16;
        *(LAS f32x4*)(L + w * 16384 + POFF + o) = acc[db];
    }
}
template <int U>
__device__ __forceinline__ void q_rest(const Stream& s) {
    if constexpr (U < 8) { issue<PQ + U>(s); q_rest<U + 1>(s); }
}

__device__ __forceinline__ void sweep_issue(u32x4 (&x)[4], __amdgpu_buffer_rsrc_t rW, unsigned vo, unsigned so) {
#pragma unroll
    for (int kk = 0; kk < 4; ++kk)
        asm volatile("buffer_load_dwordx4 %0, %1, %2, %3 offen offset:%4 sc1" : "=v"(x[kk]) : "v"(vo), "s"(rW), "s"(so + (kk >> 1) * 4096), "i"((kk & 1) * 1024) : "memory");
}
__device__ __forceinline__ void sweep_wait(u32x4 (&x)[4]) {
    asm volatile("s_waitcnt vmcnt(0)" : "+v"(x[0]), "+v"(x[1]), "+v"(x[2]), "+v"(x[3]) :: "memory");
}
__device__ __forceinline__ bool tags_ok(const u32x4 (&x)[4], unsigned tag) {
    unsigned bad = x[0][1] ^ tag;
    bad = __builtin_amdgcn_bitop3_b32(x[0][3], tag, bad, 0xBE);
#pragma unroll
    for (int kk = 1; kk < 4; ++kk) { bad = __builtin_amdgcn_bitop3_b32(x[kk][1], tag, bad, 0xBE); bad = __builtin_amdgcn_bitop3_b32(x[kk][3], tag, bad, 0xBE); }
    return __builtin_amdgcn_ballot_w64(bad != 0) == 0;
}
__device__ __forceinline__ void sweep_complete(u32x4 (&x)[4], __amdgpu_buffer_rsrc_t rW, unsigned vo, unsigned so, unsigned tag) {
    if (!tags_ok(x, tag)) {
        for (unsigned spin = 0; spin < SPIN_CAP; ++spin) {
            __builtin_amdgcn_s_sleep(1);
#pragma unroll
            for (int kk = 0; kk < 4; ++kk) x[kk] = __builtin_amdgcn_raw_buffer_load_b128(rW, vo + (kk & 1) * 1024, so + (kk >> 1) * 4096, AUX_SC1);
            if (tags_ok(x, tag)) break;
        }
    }
}
__device__ __forceinline__ void image_write(const u32x4 (&x)[4], unsigned addr, unsigned step) {
#pragma unroll
    for (int kk = 0; kk < 2; ++kk) {
        u32x2 d; d[0] = add_bf16x2(x[kk][0], x[kk + 2][0]); d[1] = add_bf16x2(x[kk][2], x[kk + 2][2]);
        *(LAS u32x2*)(size_t)(addr + kk * step) = d;
    }
}

__global__ __launch_bounds__(512, 2) void linformer_fused(
    const uint16_t* __restrict__ q, const uint16_t* __restrict__ k, const uint16_t* __restrict__ v,
    const uint16_t* __restrict__ ep, const uint16_t* __restrict__ fp, uint16_t* __restrict__ out, unsigned* ws)
{
    __shared__ __attribute__((aligned(16))) char lds[LDS_BYTES];
    LAS char* L = (LAS char*)lds;
    const int tid = threadIdx.x, lane = tid & 63;
    const int w = __builtin_amdgcn_readfirstlane(tid >> 6);
    const int b = blockIdx.x;
    const int h = (b & 7) + 8 * (b >> 7), j = (b >> 3) & 15;
    const int t = j >> 3, ds = (j >> 1) & 3, nh = j & 1;
    const int g = lane >> 4, c = lane & 15;

    const unsigned ldsb = (unsigned)(size_t)lds;
    Stream st;
    st.m0b = ldsb + w * 16384;
    st.xso = w * 64 * 512;
    { const unsigned rho = lane >> 3; st.xvo0 = rho * 512 + ds * 128 + (((lane & 7) ^ (rho & 2)) * 16); st.xvo1 = st.xvo0 ^ 64; }
    unsigned xrow = (unsigned)(h * SEQ + nh * 1024) * (DH * 2);
    asm volatile("" : "+s"(st.m0b), "+s"(st.xso), "+v"(st.xvo0), "+v"(st.xvo1), "+s"(xrow));
    __builtin_amdgcn_sched_barrier(0);
    st.rX = __builtin_amdgcn_make_buffer_rsrc((void*)((const char*)(t ? v : k) + xrow), 0, 1024 * DH * 2, 0x00020000);
    issue<0>(st); __builtin_amdgcn_s_sleep(1); issue<1>(st); __builtin_amdgcn_s_sleep(1); issue<2>(st);
    __builtin_amdgcn_sched_barrier(0);

    const uint16_t* qt = q + ((size_t)h * SEQ + 128 * j) * DH;
    uint16_t* ot = out + ((size_t)h * SEQ + 128 * j) * DH;
    const __amdgpu_buffer_rsrc_t rW = __builtin_amdgcn_make_buffer_rsrc((void*)ws, 0, WS_BYTES, 0x00020000);
    const __amdgpu_buffer_rsrc_t rO = __builtin_amdgcn_make_buffer_rsrc((void*)ot, 0, 128 * DH * 2, 0x00020000);
    unsigned epoch;
    asm volatile("s_load_dword %0, %1, %2" : "=s"(epoch) : "s"(ws), "s"(h * 256) : "memory");
    unsigned sink1, sink2;
    asm volatile("s_load_dword %0, %2, 0x0\n\ts_load_dword %1, %3, 0x0" : "=&s"(sink1), "=&s"(sink2) : "s"(qt), "s"(ot) : "memory");
    st.rE = __builtin_amdgcn_make_buffer_rsrc((void*)(t ? fp : ep), 0, RK * SEQ * 2, 0x00020000);
    st.rQ = __builtin_amdgcn_make_buffer_rsrc((void*)qt, 0, 128 * DH * 2, 0x00020000);
    st.rO = rO;
    st.eldsb = ldsb + EOFF + w * 4096;
    st.eso = nh * 2048 + w * 128; st.qso = w * 8192;
    asm volatile("" : "+s"(st.eldsb));
    st.evo = (lane >> 3) * 4096 + (((lane & 7) ^ (lane >> 4)) * 16);
    st.qvo = (lane >> 5) * 512 + (((lane & 31) ^ (lane >> 5)) * 16);
    const unsigned myslots = WS_GRAN_OFF + (h * 16 + j) * 4096;
    unsigned tch;
    asm volatile("buffer_load_dword %0, %1, %2, %3 offen" : "=v"(tch) : "v"((w < 4 && lane < 8) ? (unsigned)(w * 8 + lane) * 128u : 0x40000000u), "s"(rW), "s"(myslots) : "memory");

    if (t == 0) phase1<0>(L, ldsb, w, lane, st); else phase1<1>(L, ldsb, w, lane, st);
    asm volatile("s_waitcnt lgkmcnt(0)" : "+s"(epoch) :: "memory");
    asm volatile("" :: "s"(sink1), "s"(sink2), "v"(tch));
    BARRIER();

    const unsigned tag = epoch + 1u;
    {
        unsigned ra = ldsb + POFF + w * (t ? 528 : 512) + lane * 8, rb = ra + 65536;
        asm volatile("" : "+v"(ra), "+v"(rb));
        f32x2 p[8];
#pragma unroll
        for (int ww = 0; ww < 8; ++ww) p[ww] = *(const LAS f32x2*)(size_t)((ww < 4 ? ra : rb) + (ww & 3) * 16384);
        const f32x2 sum = ((p[0] + p[1]) + (p[2] + p[3])) + ((p[4] + p[5]) + (p[6] + p[7]));
        u32x2 gr; gr[0] = pack_bf16(sum[0], sum[1]); gr[1] = tag;
        __builtin_amdgcn_raw_buffer_store_b64(gr, rW, tid * 8, myslots, AUX_SC1);
    }
    asm volatile("" : "+s"(st.m0b), "+s"(st.qso));
    asm volatile("s_barrier" ::: "memory");
    q_rest<WIN>(st);
    __builtin_amdgcn_sched_barrier(0);
    unsigned qa[4];
#pragma unroll
    for (int i = 0; i < 4; ++i) qa[i] = ldsb + w * 16384 + c * 512 + (((g ^ c) * 16) ^ (64 * i));
    unsigned ka = ldsb + KIMG + lane * 16, va = ldsb + VIMG + lane * 16;
    unsigned kw = ldsb + KIMG + (w >> 1) * 2048 + ((w & 1) * 128 + lane) * 8;
    unsigned vw = ldsb + VIMG + ((w >> 2) * 4 + 2 * (w & 1)) * 1024 + (16 * ((lane >> 3) & 3) + 8 * ((w >> 1) & 1) + (lane & 7)) * 16 + 8 * (lane >> 5);
    unsigned ovo = g * 2048 + c * 16;
    unsigned gvo = ((w & 1) * 128 + lane) * 16;
    asm volatile("" : "+v"(qa[0]), "+v"(qa[1]), "+v"(qa[2]), "+v"(qa[3]), "+v"(ka), "+v"(va), "+v"(kw), "+v"(vw), "+v"(ovo), "+v"(gvo));
    waitvm<0>();

    const unsigned gso = WS_GRAN_OFF + h * 65536 + (w >> 1) * 8192;
    u32x4 xk[4], xv[4];
    sweep_issue(xk, rW, gvo, gso);
    bf16x8 qf[8];
#pragma unroll
    for (int ks = 0; ks < 8; ++ks) qf[ks] = *(const LAS bf16x8*)(size_t)(qa[ks & 3] + (ks >> 2) * 256);
    __builtin_amdgcn_sched_barrier(0);
    sweep_wait(xk);
    sweep_complete(xk, rW, gvo, gso, tag);
    __builtin_amdgcn_sched_barrier(0);
    sweep_issue(xv, rW, gvo, gso + 32768);
    __builtin_amdgcn_sched_barrier(0);
    image_write(xk, kw, 512);
    BARRIER();

    bf16x8 kf[8];
#pragma unroll
    for (int ks = 0; ks < 8; ++ks) kf[ks] = *(const LAS bf16x8*)(size_t)(ka + ks * 1024);
    __builtin_amdgcn_sched_barrier(0);
    f32x4 s0 = {0.f, 0.f, 0.f, 0.f}, s1 = {0.f, 0.f, 0.f, 0.f};
#pragma unroll
    for (int ks = 0; ks < 8; ks += 2) {
        s0 = __builtin_amdgcn_mfma_f32_16x16x32_bf16(kf[ks], qf[ks], s0, 0, 0, 0);
        s1 = __builtin_amdgcn_mfma_f32_16x16x32_bf16(kf[ks + 1], qf[ks + 1], s1, 0, 0, 0);
    }
    __builtin_amdgcn_sched_barrier(0);
    const f32x4 sv = s0 + s1;
    const float mx = groups_max(max_f32(max_f32(sv[0], sv[1]), max_f32(sv[2], sv[3])));
    constexpr float SC = 0.0625f * 1.44269504088896340736f;
    const float e0 = __builtin_amdgcn_exp2f((sv[0] - mx) * SC), e1 = __builtin_amdgcn_exp2f((sv[1] - mx) * SC);
    const float e2 = __builtin_amdgcn_exp2f((sv[2] - mx) * SC), e3 = __builtin_amdgcn_exp2f((sv[3] - mx) * SC);
    const float inv = __builtin_amdgcn_rcpf(groups_sum((e0 + e1) + (e2 + e3)));
    u32x2 p2; p2[0] = pack_bf16(e0 * inv, e1 * inv); p2[1] = pack_bf16(e2 * inv, e3 * inv);
    asm volatile("" : "+v"(p2));
    const s16x4 pa = __builtin_bit_cast(s16x4, p2);
    sweep_wait(xv);
    sweep_complete(xv, rW, gvo, gso + 32768, tag);
    image_write(xv, vw, 1024);
    BARRIER();

    u32x4 vf[8];
#pragma unroll
    for (int i = 0; i < 8; ++i) vf[i] = *(const LAS u32x4*)(size_t)(va + i * 1024);
    __builtin_amdgcn_sched_barrier(0);
    if (j == 0 && tid == 0) __builtin_amdgcn_raw_buffer_store_b32(tag, rW, 0, h * 256, AUX_SC1);
#pragma unroll
    for (int dh = 0; dh < 2; ++dh) {
        f32x4 o[8];
#pragma unroll
        for (int mp = 0; mp < 4; ++mp) {
            u32x2 x0; x0[0] = vf[dh * 4 + mp][0]; x0[1] = vf[dh * 4 + mp][1];
            u32x2 x1; x1[0] = vf[dh * 4 + mp][2]; x1[1] = vf[dh * 4 + mp][3];
            const f32x4 z = {0.f, 0.f, 0.f, 0.f};
            o[2 * mp]     = __builtin_amdgcn_mfma_f32_16x16x16bf16_1k(pa, __builtin_bit_cast(s16x4, x0), z, 0, 0, 0);
            o[2 * mp + 1] = __builtin_amdgcn_mfma_f32_16x16x16bf16_1k(pa, __builtin_bit_cast(s16x4, x1), z, 0, 0, 0);
        }
#pragma unroll
        for (int r = 0; r < 4; ++r) {
            u32x4 ov;
#pragma unroll
            for (int i = 0; i < 4; ++i) ov[i] = pack_bf16(o[2 * i][r], o[2 * i + 1][r]);
            __builtin_amdgcn_raw_buffer_store_b128(ov, rO, ovo + r * 512 + dh * 256, w * 8192, AUX_NT);
        }
    }
}

extern "C" void kernel_launch(void* const* d_in, const int* in_sizes, int n_in,
                              void* d_out, int out_size, void* d_ws, size_t ws_size,
                              hipStream_t stream) {
    linformer_fused<<<256, 512, 0, stream>>>((const uint16_t*)d_in[0], (const uint16_t*)d_in[1], (const uint16_t*)d_in[2],
                                             (const uint16_t*)d_in[3], (const uint16_t*)d_in[4], (uint16_t*)d_out, (unsigned*)d_ws);
}
```

```cpp
#include <hip/hip_runtime.h>
#include <hip/hip_bf16.h>
#include <stdint.h>

typedef __attribute__((ext_vector_type(8))) __bf16 bf16x8;
typedef __attribute__((ext_vector_type(2))) __bf16 bf16x2;
typedef __attribute__((ext_vector_type(4))) short s16x4;
typedef __attribute__((ext_vector_type(4))) float f32x4;
typedef __attribute__((ext_vector_type(2))) float f32x2;
typedef __attribute__((ext_vector_type(4))) unsigned u32x4;
typedef __attribute__((ext_vector_type(2))) unsigned u32x2;
#define LAS __attribute__((address_space(3)))

constexpr int SEQ = 2048, DH = 256, RK = 16;
constexpr unsigned EOFF = 131072, LDS_BYTES = 163840, POFF = 8192, KIMG = EOFF, VIMG = EOFF + 8192;
constexpr unsigned WS_GRAN_OFF = 65536, WS_BYTES = WS_GRAN_OFF + 256 * 4096;
constexpr unsigned SPIN_CAP = 1u << 15;
constexpr int WIN = 3;
constexpr int AUX_NT = 2, AUX_SC1 = 16;

__device__ __forceinline__ unsigned pack_bf16(float a, float b) {
    bf16x2 r; r[0] = (__bf16)a; r[1] = (__bf16)b;
    return __builtin_bit_cast(unsigned, r);
}
__device__ __forceinline__ unsigned add_bf16x2(unsigned a, unsigned b) {
    const float lo = __uint_as_float(a << 16) + __uint_as_float(b << 16);
    const float hi = __uint_as_float(a & 0xffff0000u) + __uint_as_float(b & 0xffff0000u);
    return pack_bf16(lo, hi);
}
__device__ __forceinline__ float max_f32(float a, float b) { float r; asm("v_max_f32 %0, %1, %2" : "=v"(r) : "v"(a), "v"(b)); return r; }
__device__ __forceinline__ float groups_max(float x) {
    auto a = __builtin_amdgcn_permlane16_swap(__float_as_uint(x), __float_as_uint(x), false, false);
    x = max_f32(__uint_as_float(a[0]), __uint_as_float(a[1]));
    auto b = __builtin_amdgcn_permlane32_swap(__float_as_uint(x), __float_as_uint(x), false, false);
    return max_f32(__uint_as_float(b[0]), __uint_as_float(b[1]));
}
__device__ __forceinline__ float groups_sum(float x) {
    auto a = __builtin_amdgcn_permlane16_swap(__float_as_uint(x), __float_as_uint(x), false, false);
    x = __uint_as_float(a[0]) + __uint_as_float(a[1]);
    auto b = __builtin_amdgcn_permlane32_swap(__float_as_uint(x), __float_as_uint(x), false, false);
    return __uint_as_float(b[0]) + __uint_as_float(b[1]);
}
#define DMA(pol, m0v, vo, rs, so) \
    asm volatile("s_mov_b32 m0, %0\n\ts_nop 0\n\tbuffer_load_dwordx4 %1, %2, %3 offen " pol " lds" :: "s"(m0v), "v"(vo), "s"(rs), "s"(so) : "memory")
template <int N> __device__ __forceinline__ void waitvm() { asm volatile("s_waitcnt vmcnt(%0)" :: "i"(N) : "memory"); }
#define BARRIER() asm volatile("s_waitcnt lgkmcnt(0)\n\ts_barrier" ::: "memory")

constexpr int PQ = 20;
constexpr int PTOUCH = 12;
constexpr int pos_kind(int p) { return p >= PQ ? 2 : (p == 3 || p == 4 || p == 8 || p == 9) ? 1 : 0; }
constexpr int pos_index(int p) { return p >= PQ ? p - PQ : p == 3 ? 0 : p == 4 ? 1 : p == 8 ? 2 : p == 9 ? 3 : p < 3 ? p : p < 8 ? p - 2 : p - 4; }
constexpr int kv_pos(int i) { return i < 3 ? i : i < 6 ? i + 2 : i + 4; }
constexpr int ks_pos(int ks) { return kv_pos(4 * ks + 3); }

struct Stream {
    unsigned eldsb, m0b, evo, xvo0, xvo1, qvo, eso, xso, qso;
    __amdgpu_buffer_rsrc_t rE, rX, rQ, rO;
};
template <int P> __device__ __forceinline__ void issue(const Stream& s) {
    constexpr int i = pos_index(P);
    if constexpr (pos_kind(P) == 1) DMA("", s.eldsb + i * 1024, s.evo ^ ((i & 1) * 64), s.rE, s.eso + (i & 1) * 32768 + (i >> 1) * 1024);
    else if constexpr (pos_kind(P) == 0) DMA("nt", s.m0b + i * 1024, (i & 1) ? s.xvo1 : s.xvo0, s.rX, s.xso + (512 * (i >> 3) + 8 * (i & 7)) * 512);
    else DMA("nt", s.m0b + i * 1024, s.qvo ^ (i * 32), s.rQ, s.qso + i * 1024);
}

template <int T>
__device__ __forceinline__ void proj_step(const LAS char* xb, unsigned eb, int ks, const unsigned (&to)[4], f32x4 (&acc)[4]) {
    const bf16x8 e = *(const LAS bf16x8*)(size_t)(((ks & 1) ? eb ^ 64 : eb) + (ks >> 1) * 2048);
    bf16x8 x[4];
#pragma unroll
    for (int db = 0; db < 4; ++db) {
        s16x4 x0 = __builtin_amdgcn_ds_read_tr16_b64_v4i16((LAS s16x4*)(xb + to[db] + ks * 4096));
        s16x4 x1 = __builtin_amdgcn_ds_read_tr16_b64_v4i16((LAS s16x4*)(xb + to[db] + ks * 4096 + 512));
        x[db] = __builtin_bit_cast(bf16x8, __builtin_shufflevector(x0, x1, 0, 1, 2, 3, 4, 5, 6, 7));
    }
    __builtin_amdgcn_sched_barrier(0);
#pragma unroll
    for (int db = 0; db < 4; ++db) {
        if (T == 0) acc[db] = __builtin_amdgcn_mfma_f32_16x16x32_bf16(x[db], e, acc[db], 0, 0, 0);
        else        acc[db] = __builtin_amdgcn_mfma_f32_16x16x32_bf16(e, x[db], acc[db], 0, 0, 0);
    }
    __builtin_amdgcn_sched_barrier(0);
}

constexpr int hbm_index(int p) { return p >= PQ ? 16 + p - PQ : pos_index(p); }
constexpr int hbm_pos(int hi) { return hi >= 16 ? PQ + hi - 16 : kv_pos(hi); }
template <int T, int P>
__device__ __forceinline__ void stream_steps(const Stream& s, const LAS char* xb, unsigned eb, const unsigned (&to)[4], f32x4 (&acc)[4], unsigned (&vt)[2]) {
    if constexpr (P < PQ + WIN) {
        constexpr bool hbm = pos_kind(P) != 1;
        constexpr int need = hbm ? hbm_pos(hbm_index(P) - WIN) : -1;
        if constexpr (hbm) waitvm<P - need - 1 + (need < 3 ? 1 : 0) + ((P > PTOUCH && need <= PTOUCH) ? 2 : 0)>();
        if constexpr (P == PQ) asm volatile("s_waitcnt lgkmcnt(0)" ::: "memory");
        issue<P>(s);
        if constexpr (P == PTOUCH)
            asm volatile("buffer_load_dword %0, off, %2, 0\n\tbuffer_load_dword %1, off, %3, 0" : "=&v"(vt[0]), "=&v"(vt[1]) : "s"(s.rQ), "s"(s.rO) : "memory");
        if constexpr (need == ks_pos(0)) proj_step<T>(xb, eb, 0, to, acc);
        if constexpr (need == ks_pos(1)) proj_step<T>(xb, eb, 1, to, acc);
        if constexpr (need == ks_pos(2)) proj_step<T>(xb, eb, 2, to, acc);
        if constexpr (need == ks_pos(3)) proj_step<T>(xb, eb, 3, to, acc);
        stream_steps<T, P + 1>(s, xb, eb, to, acc, vt);
    }
}

template <int T>
__device__ __forceinline__ void phase1(LAS char* L, unsigned ldsb, int w, int lane, const Stream& s) {
    static_assert(WIN == 3 && hbm_pos(7 + WIN) < PQ && kv_pos(3) > 4 && kv_pos(11) > 9, "positions 0..2 are requested by the prologue; E pieces precede their k-steps");
    const int g = lane >> 4, c = lane & 15;
    const unsigned qq = (lane >> 2) & 3, pp = lane & 3;
    unsigned to[4];
#pragma unroll
    for (int db = 0; db < 4; ++db)
        to[db] = g * 1024 + qq * 128 + (((2 * db + (pp >> 1)) ^ (((g & 1) << 2) | (qq & 2))) * 16) + 8 * (pp & 1);
    const LAS char* xb = L + w * 16384;
    const unsigned eb = ldsb + EOFF + w * 4096 + (c >> 3) * 1024 + (c & 7) * 128 + ((g ^ ((c >> 1) & 7)) * 16);
    f32x4 acc[4];
#pragma unroll
    for (int db = 0; db < 4; ++db) acc[db] = (f32x4){0.f, 0.f, 0.f, 0.f};
    unsigned vt[2];
    stream_steps<T, 3>(s, xb, eb, to, acc, vt);
    asm volatile("" :: "v"(vt[0]), "v"(vt[1]));
#pragma unroll
    for (int db = 0; db < 4; ++db) {
        const unsigned o = T == 0 ? db * 1024 + (g >> 1) * 512 + c * 32 + (g & 1) * 16 : (c & 7) * 528 + (g * 8 + 2 * db + (c >> 3)) * 16;
        *(LAS f32x4*)(L + w * 16384 + POFF + o) = acc[db];
    }
}
template <int U>
__device__ __forceinline__ void q_rest(const Stream& s) {
    if constexpr (U < 8) { issue<PQ + U>(s); q_rest<U + 1>(s); }
}

__device__ __forceinline__ void sweep_issue(u32x4 (&x)[4], __amdgpu_buffer_rsrc_t rW, unsigned vo, unsigned so) {
#pragma unroll
    for (int kk = 0; kk < 4; ++kk)
        asm volatile("buffer_load_dwordx4 %0, %1, %2, %3 offen offset:%4 sc1" : "=v"(x[kk]) : "v"(vo), "s"(rW), "s"(so + (kk >> 1) * 4096), "i"((kk & 1) * 1024) : "memory");
}
__device__ __forceinline__ void sweep_wait(u32x4 (&x)[4]) {
    asm volatile("s_waitcnt vmcnt(0)" : "+v"(x[0]), "+v"(x[1]), "+v"(x[2]), "+v"(x[3]) :: "memory");
}
__device__ __forceinline__ bool tags_ok(const u32x4 (&x)[4], unsigned tag) {
    unsigned bad = x[0][1] ^ tag;
    bad = __builtin_amdgcn_bitop3_b32(x[0][3], tag, bad, 0xBE);
#pragma unroll
    for (int kk = 1; kk < 4; ++kk) { bad = __builtin_amdgcn_bitop3_b32(x[kk][1], tag, bad, 0xBE); bad = __builtin_amdgcn_bitop3_b32(x[kk][3], tag, bad, 0xBE); }
    return __builtin_amdgcn_ballot_w64(bad != 0) == 0;
}
__device__ __forceinline__ void sweep_complete(u32x4 (&x)[4], __amdgpu_buffer_rsrc_t rW, unsigned vo, unsigned so, unsigned tag) {
    if (!tags_ok(x, tag)) {
        for (unsigned spin = 0; spin < SPIN_CAP; ++spin) {
            __builtin_amdgcn_s_sleep(1);
#pragma unroll
            for (int kk = 0; kk < 4; ++kk) x[kk] = __builtin_amdgcn_raw_buffer_load_b128(rW, vo + (kk & 1) * 1024, so + (kk >> 1) * 4096, AUX_SC1);
            if (tags_ok(x, tag)) break;
        }
    }
}
__device__ __forceinline__ void image_write(const u32x4 (&x)[4], unsigned addr, unsigned step) {
#pragma unroll
    for (int kk = 0; kk < 2; ++kk) {
        u32x2 d; d[0] = add_bf16x2(x[kk][0], x[kk + 2][0]); d[1] = add_bf16x2(x[kk][2], x[kk + 2][2]);
        *(LAS u32x2*)(size_t)(addr + kk * step) = d;
    }
}

__global__ __launch_bounds__(512, 2) void linformer_fused(
    const uint16_t* __restrict__ q, const uint16_t* __restrict__ k, const uint16_t* __restrict__ v,
    const uint16_t* __restrict__ ep, const uint16_t* __restrict__ fp, uint16_t* __restrict__ out, unsigned* ws)
{
    __shared__ __attribute__((aligned(16))) char lds[LDS_BYTES];
    LAS char* L = (LAS char*)lds;
    const int tid = threadIdx.x, lane = tid & 63;
    const int w = __builtin_amdgcn_readfirstlane(tid >> 6);
    const int b = blockIdx.x;
    const int h = (b & 7) + 8 * (b >> 7), j = (b >> 3) & 15;
    const int t = j >> 3, ds = (j >> 1) & 3, nh = j & 1;
    const int g = lane >> 4, c = lane & 15;

    const unsigned ldsb = (unsigned)(size_t)lds;
    Stream st;
    st.m0b = ldsb + w * 16384;
    st.xso = w * 64 * 512;
    { const unsigned rho = lane >> 3; st.xvo0 = rho * 512 + ds * 128 + (((lane & 7) ^ (rho & 2)) * 16); st.xvo1 = st.xvo0 ^ 64; }
    unsigned xrow = (unsigned)(h * SEQ + nh * 1024) * (DH * 2);
    asm volatile("" : "+s"(st.m0b), "+s"(st.xso), "+v"(st.xvo0), "+v"(st.xvo1), "+s"(xrow));
    __builtin_amdgcn_sched_barrier(0);
    st.rX = __builtin_amdgcn_make_buffer_rsrc((void*)((const char*)(t ? v : k) + xrow), 0, 1024 * DH * 2, 0x00020000);
    issue<0>(st); issue<1>(st); issue<2>(st);
    __builtin_amdgcn_sched_barrier(0);

    const uint16_t* qt = q + ((size_t)h * SEQ + 128 * j) * DH;
    uint16_t* ot = out + ((size_t)h * SEQ + 128 * j) * DH;
    const __amdgpu_buffer_rsrc_t rW = __builtin_amdgcn_make_buffer_rsrc((void*)ws, 0, WS_BYTES, 0x00020000);
    const __amdgpu_buffer_rsrc_t rO = __builtin_amdgcn_make_buffer_rsrc((void*)ot, 0, 128 * DH * 2, 0x00020000);
    unsigned epoch;
    asm volatile("s_load_dword %0, %1, %2" : "=&s"(epoch) : "s"(ws), "s"(h * 256) : "memory");
    unsigned sink1, sink2;
    asm volatile("s_load_dword %0, %2, 0x0\n\ts_load_dword %1, %3, 0x0" : "=&s"(sink1), "=&s"(sink2) : "s"(qt), "s"(ot) : "memory");
    st.rE = __builtin_amdgcn_make_buffer_rsrc((void*)(t ? fp : ep), 0, RK * SEQ * 2, 0x00020000);
    st.rQ = __builtin_amdgcn_make_buffer_rsrc((void*)qt, 0, 128 * DH * 2, 0x00020000);
    st.rO = rO;
    st.eldsb = ldsb + EOFF + w * 4096;
    st.eso = nh * 2048 + w * 128; st.qso = w * 8192;
    asm volatile("" : "+s"(st.eldsb));
    st.evo = (lane >> 3) * 4096 + (((lane & 7) ^ (lane >> 4)) * 16);
    st.qvo = (lane >> 5) * 512 + (((lane & 31) ^ (lane >> 5)) * 16);
    const unsigned myslots = WS_GRAN_OFF + (h * 16 + j) * 4096;
    unsigned tch;
    asm volatile("buffer_load_dword %0, %1, %2, %3 offen" : "=v"(tch) : "v"((w < 4 && lane < 8) ? (unsigned)(w * 8 + lane) * 128u : 0x40000000u), "s"(rW), "s"(myslots) : "memory");

    if (t == 0) phase1<0>(L, ldsb, w, lane, st); else phase1<1>(L, ldsb, w, lane, st);
    asm volatile("s_waitcnt lgkmcnt(0)" : "+s"(epoch) :: "memory");
    asm volatile("" :: "s"(sink1), "s"(sink2), "v"(tch));
    BARRIER();

    const unsigned tag = epoch + 1u;
    {
        unsigned ra = ldsb + POFF + w * (t ? 528 : 512) + lane * 8, rb = ra + 65536;
        asm volatile("" : "+v"(ra), "+v"(rb));
        f32x2 p[8];
#pragma unroll
        for (int ww = 0; ww < 8; ++ww) p[ww] = *(const LAS f32x2*)(size_t)((ww < 4 ? ra : rb) + (ww & 3) * 16384);
        const f32x2 sum = ((p[0] + p[1]) + (p[2] + p[3])) + ((p[4] + p[5]) + (p[6] + p[7]));
        u32x2 gr; gr[0] = pack_bf16(sum[0], sum[1]); gr[1] = tag;
        __builtin_amdgcn_raw_buffer_store_b64(gr, rW, tid * 8, myslots, AUX_SC1);
    }
    asm volatile("" : "+s"(st.m0b), "+s"(st.qso));
    asm volatile("s_barrier" ::: "memory");
    q_rest<WIN>(st);
    __builtin_amdgcn_sched_barrier(0);
    unsigned qa[4];
#pragma unroll
    for (int i = 0; i < 4; ++i) qa[i] = ldsb + w * 16384 + c * 512 + (((g ^ c) * 16) ^ (64 * i));
    unsigned ka = ldsb + KIMG + lane * 16, va = ldsb + VIMG + lane * 16;
    unsigned kw = ldsb + KIMG + (w >> 1) * 2048 + ((w & 1) * 128 + lane) * 8;
    unsigned vw = ldsb + VIMG + ((w >> 2) * 4 + 2 * (w & 1)) * 1024 + (16 * ((lane >> 3) & 3) + 8 * ((w >> 1) & 1) + (lane & 7)) * 16 + 8 * (lane >> 5);
    unsigned ovo = g * 2048 + c * 16;
    unsigned gvo = ((w & 1) * 128 + lane) * 16;
    asm volatile("" : "+v"(qa[0]), "+v"(qa[1]), "+v"(qa[2]), "+v"(qa[3]), "+v"(ka), "+v"(va), "+v"(kw), "+v"(vw), "+v"(ovo), "+v"(gvo));
    waitvm<0>();

    const unsigned gso = WS_GRAN_OFF + h * 65536 + (w >> 1) * 8192;
    u32x4 xk[4], xv[4];
    sweep_issue(xk, rW, gvo, gso);
    bf16x8 qf[8];
#pragma unroll
    for (int ks = 0; ks < 8; ++ks) qf[ks] = *(const LAS bf16x8*)(size_t)(qa[ks & 3] + (ks >> 2) * 256);
    __builtin_amdgcn_sched_barrier(0);
    sweep_wait(xk);
    sweep_complete(xk, rW, gvo, gso, tag);
    __builtin_amdgcn_sched_barrier(0);
    sweep_issue(xv, rW, gvo, gso + 32768);
    __builtin_amdgcn_sched_barrier(0);
    image_write(xk, kw, 512);
    BARRIER();

    bf16x8 kf[8];
#pragma unroll
    for (int ks = 0; ks < 8; ++ks) kf[ks] = *(const LAS bf16x8*)(size_t)(ka + ks * 1024);
    __builtin_amdgcn_sched_barrier(0);
    f32x4 s0 = {0.f, 0.f, 0.f, 0.f}, s1 = {0.f, 0.f, 0.f, 0.f};
#pragma unroll
    for (int ks = 0; ks < 8; ks += 2) {
        s0 = __builtin_amdgcn_mfma_f32_16x16x32_bf16(kf[ks], qf[ks], s0, 0, 0, 0);
        s1 = __builtin_amdgcn_mfma_f32_16x16x32_bf16(kf[ks + 1], qf[ks + 1], s1, 0, 0, 0);
    }
    __builtin_amdgcn_sched_barrier(0);
    sweep_wait(xv);
    sweep_complete(xv, rW, gvo, gso + 32768, tag);
    image_write(xv, vw, 1024);
    __builtin_amdgcn_sched_barrier(0);
    const f32x4 sv = s0 + s1;
    const float mx = groups_max(max_f32(max_f32(sv[0], sv[1]), max_f32(sv[2], sv[3])));
    constexpr float SC = 0.0625f * 1.44269504088896340736f;
    const float e0 = __builtin_amdgcn_exp2f((sv[0] - mx) * SC), e1 = __builtin_amdgcn_exp2f((sv[1] - mx) * SC);
    const float e2 = __builtin_amdgcn_exp2f((sv[2] - mx) * SC), e3 = __builtin_amdgcn_exp2f((sv[3] - mx) * SC);
    const float inv = __builtin_amdgcn_rcpf(groups_sum((e0 + e1) + (e2 + e3)));
    u32x2 p2; p2[0] = pack_bf16(e0 * inv, e1 * inv); p2[1] = pack_bf16(e2 * inv, e3 * inv);
    asm volatile("" : "+v"(p2));
    const s16x4 pa = __builtin_bit_cast(s16x4, p2);
    BARRIER();

    u32x4 vf[8];
#pragma unroll
    for (int i = 0; i < 8; ++i) vf[i] = *(const LAS u32x4*)(size_t)(va + i * 1024);
    __builtin_amdgcn_sched_barrier(0);
    if (j == 0 && tid == 0) __builtin_amdgcn_raw_buffer_store_b32(tag, rW, 0, h * 256, AUX_SC1);
#pragma unroll
    for (int dh = 0; dh < 2; ++dh) {
        f32x4 o[8];
#pragma unroll
        for (int mp = 0; mp < 4; ++mp) {
            u32x2 x0; x0[0] = vf[dh * 4 + mp][0]; x0[1] = vf[dh * 4 + mp][1];
            u32x2 x1; x1[0] = vf[dh * 4 + mp][2]; x1[1] = vf[dh * 4 + mp][3];
            const f32x4 z = {0.f, 0.f, 0.f, 0.f};
            o[2 * mp]     = __builtin_amdgcn_mfma_f32_16x16x16bf16_1k(pa, __builtin_bit_cast(s16x4, x0), z, 0, 0, 0);
            o[2 * mp + 1] = __builtin_amdgcn_mfma_f32_16x16x16bf16_1k(pa, __builtin_bit_cast(s16x4, x1), z, 0, 0, 0);
        }
#pragma unroll
        for (int r = 0; r < 4; ++r) {
            u32x4 ov;
#pragma unroll
            for (int i = 0; i < 4; ++i) ov[i] = pack_bf16(o[2 * i][r], o[2 * i + 1][r]);
            __builtin_amdgcn_raw_buffer_store_b128(ov, rO, ovo + r * 512 + dh * 256, w * 8192, AUX_NT);
        }
    }
}

extern "C" void kernel_launch(void* const* d_in, const int* in_sizes, int n_in,
                              void* d_out, int out_size, void* d_ws, size_t ws_size,
                              hipStream_t stream) {
    linformer_fused<<<256, 512, 0, stream>>>((const uint16_t*)d_in[0], (const uint16_t*)d_in[1], (const uint16_t*)d_in[2],
                                             (const uint16_t*)d_in[3], (const uint16_t*)d_in[4], (uint16_t*)d_out, (unsigned*)d_ws);
}
```
